```python
import torch

def _softmax(x, axis=-1):
    return torch.softmax(x, dim=axis)
def _gelu(x):
    return torch.nn.functional.gelu(x, approximate="tanh")
def _silu(x):
    return torch.nn.functional.silu(x)
def _sigmoid(x):
    return torch.sigmoid(x)
def _layernorm(x, axis=-1, eps=1e-5):
    m = x.mean(dim=axis, keepdim=True)
    v = x.var(dim=axis, keepdim=True, unbiased=False)
    return (x - m) / torch.sqrt(v + eps)
def _scatter_add(tgt, idx, src, axis=0):
    return tgt.clone().index_add_(axis, idx, src)
def _segment_sum(src, seg, n):
    out = torch.zeros((n,) + src.shape[1:], dtype=src.dtype)
    return out.index_add_(0, seg, src)
def _pad(x, pw):
    flat = tuple(v for pair in reversed(pw) for v in pair)
    return torch.nn.functional.pad(x, flat)

kernel_name = 'parallel_sum__ssm_conv_s4__mixer_channel_mlp__prompt'

def setup_inputs():

    _w = (lambda a: (lambda t: t.to(torch.bfloat16) if t.is_floating_point() else t)(torch.as_tensor(a)))
    def _A_setup():
        B, L, D, N = 1, 2048, 512, 128
        A_re = -(lambda _x: torch.exp(torch.as_tensor(_x)))((lambda *s: torch.randn(*s, dtype=torch.bfloat16) * 3.2)(D, N))
        A_im = torch.pi * (lambda *s: torch.randn(*s, dtype=torch.bfloat16) * 0.05)(D, N)
        B_re = (lambda *s: torch.randn(*s, dtype=torch.bfloat16) * 0.1)(D, N)
        B_im = (lambda *s: torch.randn(*s, dtype=torch.bfloat16) * 0.1)(D, N)
        C_re = (lambda *s: torch.randn(*s, dtype=torch.bfloat16) * 0.1)(D, N)
        C_im = (lambda *s: torch.randn(*s, dtype=torch.bfloat16) * 0.1)(D, N)
        log_dt = (lambda *s: torch.randn(*s, dtype=torch.bfloat16) * 0.05)(D) * 0.5 - 4.0
        Dskip = (lambda *s: torch.randn(*s, dtype=torch.bfloat16) * 0.05)(D)
        return {
            "u": _w((lambda *s: torch.randn(*s, dtype=torch.bfloat16) * 0.05)(B, L, D)),
            "A_re": _w(A_re),
            "A_im": _w(A_im),
            "B_re": _w(B_re),
            "B_im": _w(B_im),
            "C_re": _w(C_re),
            "C_im": _w(C_im),
            "log_dt": _w(log_dt),
            "Dskip": _w(Dskip),
        }
    _A_in = _A_setup()

    def _B_setup():
        B, S, D, F = 1, 2048, 512, 1792
        return {
            "x": _w((lambda *s: torch.randn(*s, dtype=torch.bfloat16) * 0.05)(B, S, D)),
            "w1": _w((lambda *s: torch.randn(*s, dtype=torch.bfloat16) * 1.6)(D, F) * (D ** -0.5)),
            "b1": _w((lambda *s: torch.randn(*s, dtype=torch.bfloat16) * 0.8)(F)),
            "w2": _w((lambda *s: torch.randn(*s, dtype=torch.bfloat16) * 0.05)(F, D) * (F ** -0.5)),
            "b2": _w((lambda *s: torch.randn(*s, dtype=torch.bfloat16) * 0.05)(D)),
        }
    _B_in = _B_setup()

    return {
        "A_u": _A_in["u"],
        "A_A_re": _A_in["A_re"],
        "A_A_im": _A_in["A_im"],
        "A_B_re": _A_in["B_re"],
        "A_B_im": _A_in["B_im"],
        "A_C_re": _A_in["C_re"],
        "A_C_im": _A_in["C_im"],
        "A_log_dt": _A_in["log_dt"],
        "A_Dskip": _A_in["Dskip"],
        "B_x": _B_in["x"],
        "B_w1": _B_in["w1"],
        "B_b1": _B_in["b1"],
        "B_w2": _B_in["w2"],
        "B_b2": _B_in["b2"],
    }

def reference(A_u, A_A_re, A_A_im, A_B_re, A_B_im, A_C_re, A_C_im, A_log_dt, A_Dskip, B_x, B_w1, B_b1, B_w2, B_b2):
    def _A_ref(u, A_re, A_im, B_re, B_im, C_re, C_im, log_dt, Dskip):
        Bsz, L, D = u.shape
        dt = torch.exp(log_dt)[:, None]
        A = A_re + 1j * A_im
        Bc = B_re + 1j * B_im
        Cc = C_re + 1j * C_im
        logdA = dt * A
        dB = dt * Bc
        l = (lambda x, dt: x.to(dt))(torch.arange(L), A_re.dtype)
        P = torch.exp(logdA[:, :, None] * l[None, None, :])
        K = torch.einsum("dn,dnl,dn->dl", Cc, P, dB).real
        L2 = 2 * L
        Kp = _pad(K, ((0, 0), (0, L)))
        up = (lambda x, *d: x.permute(*d))(u, 0, 2, 1)
        up = _pad(up, ((0, 0), (0, 0), (0, L)))
        Kf = (lambda x, axes=(-2,-1): torch.fft.rfft2(x.float(), dim=axes))(Kp[:, None, :], axes=(-2, -1))
        Uf = (lambda x, axes=(-2,-1): torch.fft.rfft2(x.float(), dim=axes))(up[:, :, None, :], axes=(-2, -1))
        Yf = Uf * Kf[None, :, :, :]
        y = (lambda x, s, axes=(-2,-1): torch.fft.irfft2(x, s=s, dim=axes).to(torch.bfloat16))(Yf, (1, L2), axes=(-2, -1))[:, :, 0, :L]
        y = (lambda x, *d: x.permute(*d))(y, 0, 2, 1) + u * Dskip[None, None, :]
        return y

    def _B_ref(x, w1, b1, w2, b2):
        y = _layernorm(x, axis=-1)
        h = _gelu(torch.einsum("bsd,df->bsf", y, w1) + b1)
        return torch.einsum("bsf,fd->bsd", h, w2) + b2

    x = A_u
    xn = _layernorm(x, axis=-1)
    return x + _A_ref(xn, A_A_re, A_A_im, A_B_re, A_B_im, A_C_re, A_C_im, A_log_dt, A_Dskip) + _B_ref(xn, B_w1, B_b1, B_w2, B_b2)
```

```cpp
#include <hip/hip_runtime.h>
#include <hip/hip_bf16.h>
#include <stdint.h>

typedef __attribute__((ext_vector_type(8))) short bf16x8;
typedef __attribute__((ext_vector_type(4))) float f32x4;
typedef __attribute__((ext_vector_type(16))) float f32x16;
typedef unsigned short u16;

static constexpr int L_ = 2048, D_ = 512, NS_ = 128, F_ = 1792;

__device__ __forceinline__ float bf2f(u16 v) { return __uint_as_float(((unsigned)v) << 16); }
__device__ __forceinline__ u16 f2bf(float f) {
  unsigned u = __float_as_uint(f);
  u += 0x7fffu + ((u >> 16) & 1u);
  return (u16)(u >> 16);
}
__device__ __forceinline__ float rbf(float f) { return bf2f(f2bf(f)); }

__device__ __forceinline__ float wave_sum(float v) {
#pragma unroll
  for (int o = 32; o > 0; o >>= 1) v += __shfl_xor(v, o, 64);
  return v;
}

struct Params {
  const u16* x;
  const u16* A_re; const u16* A_im; const u16* B_re; const u16* B_im; const u16* C_re; const u16* C_im;
  const u16* log_dt; const u16* Dskip;
  const u16* w1; const u16* b1; const u16* w2; const u16* b2;
  u16* out;
  u16* y2;
  u16* xnT;
  u16* Khi;
  u16* Klo;
  u16* w1T;
  u16* w2T;
  u16* h;
  u16* yAT;
};

static constexpr int KA_LN_BLOCKS = 64;
static constexpr int KA_K_BLOCKS = 512;
static constexpr int KA_T1_BLOCKS = 8 * 28;
static constexpr int KA_T2_BLOCKS = 28 * 8;

__device__ void ln_role(const Params& p, int blk, char* smem) {
  u16 (*tile)[514] = (u16 (*)[514])smem;
  const int tid = threadIdx.x, lane = tid & 63, wave = tid >> 6;
  const int t0 = blk * 32;
  for (int rr = 0; rr < 8; ++rr) {
    const int r = wave * 8 + rr;
    const int t = t0 + r;
    uint4 raw = *(const uint4*)(p.x + (size_t)t * D_ + lane * 8);
    float v[8];
    const unsigned w[4] = {raw.x, raw.y, raw.z, raw.w};
#pragma unroll
    for (int i = 0; i < 4; ++i) { v[2*i] = bf2f((u16)(w[i] & 0xffff)); v[2*i+1] = bf2f((u16)(w[i] >> 16)); }
    float s = 0.f;
#pragma unroll
    for (int i = 0; i < 8; ++i) s += v[i];
    s = wave_sum(s);
    float mean = s * (1.0f / 512.0f);
    float q = 0.f;
#pragma unroll
    for (int i = 0; i < 8; ++i) { float dlt = v[i] - mean; q += dlt * dlt; }
    q = wave_sum(q);
    float m_b = rbf(mean);
    float var_b = rbf(q * (1.0f / 512.0f));
    float den = rbf(sqrtf(rbf(var_b + 1e-5f)));
    float xn[8];
#pragma unroll
    for (int i = 0; i < 8; ++i) xn[i] = rbf(rbf(v[i] - m_b) / den);
    s = 0.f;
#pragma unroll
    for (int i = 0; i < 8; ++i) s += xn[i];
    s = wave_sum(s);
    mean = s * (1.0f / 512.0f);
    q = 0.f;
#pragma unroll
    for (int i = 0; i < 8; ++i) { float dlt = xn[i] - mean; q += dlt * dlt; }
    q = wave_sum(q);
    m_b = rbf(mean);
    var_b = rbf(q * (1.0f / 512.0f));
    den = rbf(sqrtf(rbf(var_b + 1e-5f)));
    unsigned o[4];
#pragma unroll
    for (int i = 0; i < 4; ++i) {
      u16 a = f2bf(rbf(xn[2*i] - m_b) / den);
      u16 b = f2bf(rbf(xn[2*i+1] - m_b) / den);
      o[i] = (unsigned)a | ((unsigned)b << 16);
    }
    *(uint4*)(p.y2 + (size_t)t * D_ + lane * 8) = make_uint4(o[0], o[1], o[2], o[3]);
#pragma unroll
    for (int i = 0; i < 8; ++i) tile[r][lane * 8 + i] = f2bf(xn[i]);
  }
  __syncthreads();
  for (int d = tid; d < D_; d += 256) {
    unsigned o[16];
#pragma unroll
    for (int i = 0; i < 16; ++i) o[i] = (unsigned)tile[2*i][d] | ((unsigned)tile[2*i+1][d] << 16);
    uint4* dst = (uint4*)(p.xnT + (size_t)d * L_ + t0);
    dst[0] = make_uint4(o[0], o[1], o[2], o[3]);
    dst[1] = make_uint4(o[4], o[5], o[6], o[7]);
    dst[2] = make_uint4(o[8], o[9], o[10], o[11]);
    dst[3] = make_uint4(o[12], o[13], o[14], o[15]);
  }
}

__device__ __forceinline__ float hi_l(int hidx) {
  if (hidx < 16) return (float)(16 * hidx);
  if (hidx < 24) return (float)(256 + 32 * (hidx - 16));
  if (hidx < 32) return (float)(512 + 64 * (hidx - 24));
  if (hidx < 40) return (float)(1024 + 128 * (hidx - 32));
  return 2048.f;
}

__device__ __forceinline__ void cexpf_fast(float zr, float zi, float l, float& er, float& ei) {
  float a = zr * l;
  float th = zi * l;
  float m = __expf(a);
  float s, c;
  __sincosf(th, &s, &c);
  er = m * c; ei = m * s;
}

__device__ void kgen_role(const Params& p, int d, char* smem) {
  float2* H = (float2*)smem;
  float2* zs = (float2*)(smem + 128 * 41 * 8);
  float2* ws = zs + 128;
  float* red = (float*)(ws + 128);
  float* Kd = red + 4 * 1040;
  const int tid = threadIdx.x, lane = tid & 63, wave = tid >> 6;
  const float dt = rbf(expf(bf2f(p.log_dt[d])));
  if (tid < 128) {
    const int n = tid;
    const size_t o = (size_t)d * NS_ + n;
    float ar = bf2f(p.A_re[o]), ai = bf2f(p.A_im[o]);
    float br = bf2f(p.B_re[o]), bi = bf2f(p.B_im[o]);
    float cr = bf2f(p.C_re[o]), ci = bf2f(p.C_im[o]);
    float zr = dt * ar, zi = dt * ai;
    float dbr = dt * br, dbi = dt * bi;
    zs[n] = make_float2(zr, zi);
    ws[n] = make_float2(cr * dbr - ci * dbi, cr * dbi + ci * dbr);
  }
  __syncthreads();
  for (int e = tid; e < 128 * 41; e += 256) {
    int n = e / 41, hidx = e % 41;
    float2 z = zs[n], w = ws[n];
    float er, ei;
    cexpf_fast(z.x, z.y, hi_l(hidx), er, ei);
    H[e] = make_float2(w.x * er - w.y * ei, w.x * ei + w.y * er);
  }
  __syncthreads();
  const int seg = lane >> 4, b = lane & 15;
  const float lo_l = (float)((1 << seg) * b);
  const int hbase = (seg == 0) ? 0 : (8 + 8 * seg);
  const int hcnt = (seg == 0) ? 16 : 8;
  float acc[16];
#pragma unroll
  for (int a = 0; a < 16; ++a) acc[a] = 0.f;
  float acc40 = 0.f;
  for (int nn = 0; nn < 32; ++nn) {
    const int n = wave * 32 + nn;
    float2 z = zs[n];
    float gr, gi;
    cexpf_fast(z.x, z.y, lo_l, gr, gi);
    const float2* Hn = H + n * 41 + hbase;
#pragma unroll
    for (int a = 0; a < 16; ++a) {
      float2 hv = Hn[a < hcnt ? a : 0];
      acc[a] += hv.x * gr - hv.y * gi;
    }
    acc40 += H[n * 41 + 40].x;
  }
#pragma unroll
  for (int a = 0; a < 16; ++a) red[wave * 1040 + (seg * 16 + a) * 16 + b] = acc[a];
  if (lane == 0) red[wave * 1040 + 1024] = acc40;
  __syncthreads();
  for (int e = tid; e < 1025; e += 256) Kd[e] = red[e] + red[1040 + e] + red[2 * 1040 + e] + red[3 * 1040 + e];
  __syncthreads();
  {
    const int l0 = tid * 8;
    unsigned oh[4], ol[4];
#pragma unroll
    for (int i = 0; i < 8; ++i) {
      const int l = l0 + i;
      const int lb = (int)rbf((float)l);
      int idx;
      if (lb < 256) idx = lb;
      else if (lb < 512) idx = 256 + ((lb - 256) >> 1);
      else if (lb < 1024) idx = 512 + ((lb - 512) >> 2);
      else if (lb < 2048) idx = 768 + ((lb - 1024) >> 3);
      else idx = -1;
      float kv;
      if (idx < 0) kv = Kd[1024];
      else {
        int sg = idx >> 8, q = idx & 255;
        kv = Kd[sg * 256 + q];
      }
      u16 hi = f2bf(kv);
      u16 lo = f2bf(kv - bf2f(hi));
      if (i & 1) { oh[i >> 1] |= (unsigned)hi << 16; ol[i >> 1] |= (unsigned)lo << 16; }
      else { oh[i >> 1] = hi; ol[i >> 1] = lo; }
    }
    *(uint4*)(p.Khi + (size_t)d * L_ + l0) = make_uint4(oh[0], oh[1], oh[2], oh[3]);
    *(uint4*)(p.Klo + (size_t)d * L_ + l0) = make_uint4(ol[0], ol[1], ol[2], ol[3]);
  }
}

__device__ void transpose_role(const u16* src, u16* dst, int R, int C, int r0, int c0, char* smem) {
  u16 (*tile)[66] = (u16 (*)[66])smem;
  const int tid = threadIdx.x;
  for (int i = tid; i < 64 * 8; i += 256) {
    int r = i >> 3, c8 = (i & 7) * 8;
    uint4 v = *(const uint4*)(src + (size_t)(r0 + r) * C + c0 + c8);
    const unsigned w[4] = {v.x, v.y, v.z, v.w};
#pragma unroll
    for (int k = 0; k < 4; ++k) { tile[r][c8 + 2*k] = (u16)(w[k] & 0xffff); tile[r][c8 + 2*k + 1] = (u16)(w[k] >> 16); }
  }
  __syncthreads();
  for (int i = tid; i < 64 * 8; i += 256) {
    int c = i >> 3, r8 = (i & 7) * 8;
    unsigned o[4];
#pragma unroll
    for (int k = 0; k < 4; ++k) o[k] = (unsigned)tile[r8 + 2*k][c] | ((unsigned)tile[r8 + 2*k + 1][c] << 16);
    *(uint4*)(dst + (size_t)(c0 + c) * R + r0 + r8) = make_uint4(o[0], o[1], o[2], o[3]);
  }
}

__global__ void __launch_bounds__(256) kernelA(Params p) {
  __shared__ __attribute__((aligned(16))) char smem[65536];
  int blk = blockIdx.x;
  if (blk < KA_LN_BLOCKS) { ln_role(p, blk, smem); return; }
  blk -= KA_LN_BLOCKS;
  if (blk < KA_K_BLOCKS) { kgen_role(p, blk, smem); return; }
  blk -= KA_K_BLOCKS;
  if (blk < KA_T1_BLOCKS) { transpose_role(p.w1, p.w1T, D_, F_, (blk / 28) * 64, (blk % 28) * 64, smem); return; }
  blk -= KA_T1_BLOCKS;
  transpose_role(p.w2, p.w2T, F_, D_, (blk / 8) * 64, (blk % 8) * 64, smem);
}

static constexpr int KS_ = 2192;
static constexpr int CONV_K_BYTES = 8 * KS_ * 2;
static constexpr int CONV_U_OFF = 2 * CONV_K_BYTES;
static constexpr int CONV_LDS = CONV_U_OFF + 4096 * 2;

__device__ void conv_role(const Params& p, int d, char* smem) {
  const int tid = threadIdx.x, lane = tid & 63, wave = tid >> 6;
  u16* U = (u16*)(smem + CONV_U_OFF);
  for (int i = tid; i < 512; i += 256) {
    uint4 v = (i < 256) ? make_uint4(0, 0, 0, 0) : *(const uint4*)(p.xnT + (size_t)d * L_ + (i - 256) * 8);
    *(uint4*)(U + i * 8) = v;
  }
  for (int it = tid; it < 2 * 262; it += 256) {
    const int which = it >= 262;
    const int q = which ? it - 262 : it;
    const u16* Ksrc = (which ? p.Klo : p.Khi) + (size_t)d * L_;
    unsigned w[8];
    {
      int ch = 255 - q;
      uint4 v = (ch >= 0) ? *(const uint4*)(Ksrc + ch * 8) : make_uint4(0, 0, 0, 0);
      w[0] = __builtin_amdgcn_alignbit(v.w, v.w, 16);
      w[1] = __builtin_amdgcn_alignbit(v.z, v.z, 16);
      w[2] = __builtin_amdgcn_alignbit(v.y, v.y, 16);
      w[3] = __builtin_amdgcn_alignbit(v.x, v.x, 16);
      ch = 254 - q;
      v = (ch >= 0) ? *(const uint4*)(Ksrc + ch * 8) : make_uint4(0, 0, 0, 0);
      w[4] = __builtin_amdgcn_alignbit(v.w, v.w, 16);
      w[5] = __builtin_amdgcn_alignbit(v.z, v.z, 16);
      w[6] = __builtin_amdgcn_alignbit(v.y, v.y, 16);
      w[7] = __builtin_amdgcn_alignbit(v.x, v.x, 16);
    }
    char* base = smem + which * CONV_K_BYTES + q * 16;
#pragma unroll
    for (int r = 0; r < 8; ++r) {
      uint4 o;
      const int k = r >> 1;
      if ((r & 1) == 0) o = make_uint4(w[k], w[k + 1], w[k + 2], w[k + 3]);
      else o = make_uint4(__builtin_amdgcn_alignbit(w[k + 1], w[k], 16), __builtin_amdgcn_alignbit(w[k + 2], w[k + 1], 16),
                          __builtin_amdgcn_alignbit(w[k + 3], w[k + 2], 16), __builtin_amdgcn_alignbit(w[k + 4], w[k + 3], 16));
      *(uint4*)(base + r * (KS_ * 2)) = o;
    }
  }
  __syncthreads();
  const int b = lane & 31, hh = lane >> 5;
  const int r = (7 - b) & 7;
  f32x16 acc0, acc1;
#pragma unroll
  for (int i = 0; i < 16; ++i) { acc0[i] = 0.f; acc1[i] = 0.f; }
  const char* abase = smem + r * (KS_ * 2) + (2047 - b - r + 8 * hh) * 2;
  const u16* ub0 = U + 2048 + 32 * b + 8 * hh;
  const u16* ub1 = U + 2048 + 32 * (32 + b) + 8 * hh;
  for (int j = wave; j < 64; j += 4) {
#pragma unroll
    for (int half = 0; half < 2; ++half) {
      bf16x8 ahi = *(const bf16x8*)(abase - 64 * j + 32 * half);
      bf16x8 alo = *(const bf16x8*)(abase - 64 * j + 32 * half + CONV_K_BYTES);
      bf16x8 b1v = *(const bf16x8*)(ub1 - 32 * j + 16 * half);
      acc1 = __builtin_amdgcn_mfma_f32_32x32x16_bf16(ahi, b1v, acc1, 0, 0, 0);
      acc1 = __builtin_amdgcn_mfma_f32_32x32x16_bf16(alo, b1v, acc1, 0, 0, 0);
      if (j < 32) {
        bf16x8 b0v = *(const bf16x8*)(ub0 - 32 * j + 16 * half);
        acc0 = __builtin_amdgcn_mfma_f32_32x32x16_bf16(ahi, b0v, acc0, 0, 0, 0);
        acc0 = __builtin_amdgcn_mfma_f32_32x32x16_bf16(alo, b0v, acc0, 0, 0, 0);
      }
    }
  }
  __syncthreads();
  float* red = (float*)smem;
#pragma unroll
  for (int reg = 0; reg < 16; ++reg) {
    int row = (reg & 3) + 8 * (reg >> 2) + 4 * hh;
    red[(wave * 64 + b) * 33 + row] = acc0[reg];
    red[(wave * 64 + 32 + b) * 33 + row] = acc1[reg];
  }
  __syncthreads();
  const float dsk = bf2f(p.Dskip[d]);
  {
    const int t0 = tid * 8;
    unsigned o[4];
#pragma unroll
    for (int i = 0; i < 8; ++i) {
      const int t = t0 + i, a = t >> 5, bb = t & 31;
      float s = red[a * 33 + bb] + red[(64 + a) * 33 + bb] + red[(128 + a) * 33 + bb] + red[(192 + a) * 33 + bb];
      float u = bf2f(U[2048 + t]);
      u16 y = f2bf(rbf(s) + rbf(u * dsk));
      if (i & 1) o[i >> 1] |= (unsigned)y << 16; else o[i >> 1] = y;
    }
    *(uint4*)(p.yAT + (size_t)d * L_ + t0) = make_uint4(o[0], o[1], o[2], o[3]);
  }
}

__device__ __forceinline__ float gelu_tanh(float x) {
  const float k0 = 0.7978845608028654f, k1 = 0.044715f;
  float inner = k0 * (x + k1 * x * x * x);
  return 0.5f * x * (1.0f + tanhf(inner));
}

static constexpr int G1_LDR = 72;
__device__ void gemm1_role(const Params& p, int blk, char* smem) {
  const int tid = threadIdx.x, lane = tid & 63, wave = tid >> 6;
  const int m0 = (blk / 14) * 128, n0 = (blk % 14) * 128;
  u16* sA = (u16*)smem;
  u16* sB = sA + 2 * 128 * G1_LDR;
  const int wm = (wave >> 1) * 64, wn = (wave & 1) * 64;
  const int r = lane & 31, hh = lane >> 5;
  f32x16 acc[2][2];
#pragma unroll
  for (int i = 0; i < 2; ++i)
#pragma unroll
    for (int j = 0; j < 2; ++j)
#pragma unroll
      for (int k = 0; k < 16; ++k) acc[i][j][k] = 0.f;
  uint4 ra[4], rb[4];
  const u16* Ag = p.y2 + (size_t)m0 * D_;
  const u16* Bg = p.w1T + (size_t)n0 * D_;
#pragma unroll
  for (int i = 0; i < 4; ++i) {
    int c = tid + i * 256, row = c >> 3, col = (c & 7) * 8;
    ra[i] = *(const uint4*)(Ag + (size_t)row * D_ + col);
    rb[i] = *(const uint4*)(Bg + (size_t)row * D_ + col);
  }
#pragma unroll
  for (int i = 0; i < 4; ++i) {
    int c = tid + i * 256, row = c >> 3, col = (c & 7) * 8;
    *(uint4*)(sA + row * G1_LDR + col) = ra[i];
    *(uint4*)(sB + row * G1_LDR + col) = rb[i];
  }
  __syncthreads();
  const int NK = D_ / 64;
  for (int kt = 0; kt < NK; ++kt) {
    const int cur = kt & 1;
    if (kt + 1 < NK) {
#pragma unroll
      for (int i = 0; i < 4; ++i) {
        int c = tid + i * 256, row = c >> 3, col = (c & 7) * 8;
        ra[i] = *(const uint4*)(Ag + (size_t)row * D_ + (kt + 1) * 64 + col);
        rb[i] = *(const uint4*)(Bg + (size_t)row * D_ + (kt + 1) * 64 + col);
      }
    }
    const u16* cA = sA + cur * 128 * G1_LDR;
    const u16* cB = sB + cur * 128 * G1_LDR;
#pragma unroll
    for (int ks = 0; ks < 4; ++ks) {
      bf16x8 a0 = *(const bf16x8*)(cA + (wm + r) * G1_LDR + ks * 16 + hh * 8);
      bf16x8 a1 = *(const bf16x8*)(cA + (wm + 32 + r) * G1_LDR + ks * 16 + hh * 8);
      bf16x8 b0 = *(const bf16x8*)(cB + (wn + r) * G1_LDR + ks * 16 + hh * 8);
      bf16x8 b1 = *(const bf16x8*)(cB + (wn + 32 + r) * G1_LDR + ks * 16 + hh * 8);
      acc[0][0] = __builtin_amdgcn_mfma_f32_32x32x16_bf16(a0, b0, acc[0][0], 0, 0, 0);
      acc[0][1] = __builtin_amdgcn_mfma_f32_32x32x16_bf16(a0, b1, acc[0][1], 0, 0, 0);
      acc[1][0] = __builtin_amdgcn_mfma_f32_32x32x16_bf16(a1, b0, acc[1][0], 0, 0, 0);
      acc[1][1] = __builtin_amdgcn_mfma_f32_32x32x16_bf16(a1, b1, acc[1][1], 0, 0, 0);
    }
    if (kt + 1 < NK) {
      u16* nA = sA + (cur ^ 1) * 128 * G1_LDR;
      u16* nB = sB + (cur ^ 1) * 128 * G1_LDR;
#pragma unroll
      for (int i = 0; i < 4; ++i) {
        int c = tid + i * 256, row = c >> 3, col = (c & 7) * 8;
        *(uint4*)(nA + row * G1_LDR + col) = ra[i];
        *(uint4*)(nB + row * G1_LDR + col) = rb[i];
      }
    }
    __syncthreads();
  }
  u16* sC = (u16*)smem;
  const int LDC = 136;
#pragma unroll
  for (int j = 0; j < 2; ++j) {
    const int col = wn + j * 32 + r;
    const float bias = bf2f(p.b1[n0 + col]);
#pragma unroll
    for (int i = 0; i < 2; ++i)
#pragma unroll
      for (int reg = 0; reg < 16; ++reg) {
        int row = wm + i * 32 + (reg & 3) + 8 * (reg >> 2) + 4 * hh;
        float v = rbf(rbf(acc[i][j][reg]) + bias);
        sC[row * LDC + col] = f2bf(gelu_tanh(v));
      }
  }
  __syncthreads();
  for (int c = tid; c < 128 * 16; c += 256) {
    int row = c >> 4, col = (c & 15) * 8;
    *(uint4*)(p.h + (size_t)(m0 + row) * F_ + n0 + col) = *(const uint4*)(sC + row * LDC + col);
  }
}

static constexpr int KB_CONV_BLOCKS = 512;
static constexpr int KB_G1_BLOCKS = 16 * 14;
__global__ void __launch_bounds__(256) kernelB(Params p) {
  __shared__ __attribute__((aligned(16))) char smem[CONV_LDS];
  int blk = blockIdx.x;
  if (blk < KB_G1_BLOCKS) { gemm1_role(p, blk, smem); return; }
  blk -= KB_G1_BLOCKS;
  conv_role(p, blk, smem);
}

__global__ void __launch_bounds__(256) kernelC(Params p) {
  __shared__ __attribute__((aligned(16))) u16 smem[2 * 2 * 64 * G1_LDR];
  const int tid = threadIdx.x, lane = tid & 63, wave = tid >> 6;
  const int m0 = (blockIdx.x >> 3) * 64, n0 = (blockIdx.x & 7) * 64;
  u16* sA = smem;
  u16* sB = smem + 2 * 64 * G1_LDR;
  const int wm = (wave >> 1) * 32, wn = (wave & 1) * 32;
  const int r = lane & 31, hh = lane >> 5;
  f32x16 acc;
#pragma unroll
  for (int k = 0; k < 16; ++k) acc[k] = 0.f;
  uint4 ra[2], rb[2];
  const u16* Ag = p.h + (size_t)m0 * F_;
  const u16* Bg = p.w2T + (size_t)n0 * F_;
#pragma unroll
  for (int i = 0; i < 2; ++i) {
    int c = tid + i * 256, row = c >> 3, col = (c & 7) * 8;
    ra[i] = *(const uint4*)(Ag + (size_t)row * F_ + col);
    rb[i] = *(const uint4*)(Bg + (size_t)row * F_ + col);
  }
#pragma unroll
  for (int i = 0; i < 2; ++i) {
    int c = tid + i * 256, row = c >> 3, col = (c & 7) * 8;
    *(uint4*)(sA + row * G1_LDR + col) = ra[i];
    *(uint4*)(sB + row * G1_LDR + col) = rb[i];
  }
  __syncthreads();
  const int NK = F_ / 64;
  for (int kt = 0; kt < NK; ++kt) {
    const int cur = kt & 1;
    if (kt + 1 < NK) {
#pragma unroll
      for (int i = 0; i < 2; ++i) {
        int c = tid + i * 256, row = c >> 3, col = (c & 7) * 8;
        ra[i] = *(const uint4*)(Ag + (size_t)row * F_ + (kt + 1) * 64 + col);
        rb[i] = *(const uint4*)(Bg + (size_t)row * F_ + (kt + 1) * 64 + col);
      }
    }
    const u16* cA = sA + cur * 64 * G1_LDR;
    const u16* cB = sB + cur * 64 * G1_LDR;
#pragma unroll
    for (int ks = 0; ks < 4; ++ks) {
      bf16x8 a0 = *(const bf16x8*)(cA + (wm + r) * G1_LDR + ks * 16 + hh * 8);
      bf16x8 b0 = *(const bf16x8*)(cB + (wn + r) * G1_LDR + ks * 16 + hh * 8);
      acc = __builtin_amdgcn_mfma_f32_32x32x16_bf16(a0, b0, acc, 0, 0, 0);
    }
    if (kt + 1 < NK) {
      u16* nA = sA + (cur ^ 1) * 64 * G1_LDR;
      u16* nB = sB + (cur ^ 1) * 64 * G1_LDR;
#pragma unroll
      for (int i = 0; i < 2; ++i) {
        int c = tid + i * 256, row = c >> 3, col = (c & 7) * 8;
        *(uint4*)(nA + row * G1_LDR + col) = ra[i];
        *(uint4*)(nB + row * G1_LDR + col) = rb[i];
      }
    }
    __syncthreads();
  }
  const int dd = n0 + wn + r;
  const float bias = bf2f(p.b2[dd]);
#pragma unroll
  for (int g = 0; g < 4; ++g) {
    const int t = m0 + wm + 8 * g + 4 * hh;
    uint2 ya = *(const uint2*)(p.yAT + (size_t)dd * L_ + t);
    const unsigned yw[2] = {ya.x, ya.y};
#pragma unroll
    for (int i = 0; i < 4; ++i) {
      float yA = bf2f((u16)((yw[i >> 1] >> (16 * (i & 1))) & 0xffff));
      float xv = bf2f(p.x[(size_t)(t + i) * D_ + dd]);
      float yB = rbf(rbf(acc[g * 4 + i]) + bias);
      p.out[(size_t)(t + i) * D_ + dd] = f2bf(rbf(xv + yA) + yB);
    }
  }
}

extern "C" void kernel_launch(void* const* d_in, const int* in_sizes, int n_in,
                              void* d_out, int out_size, void* d_ws, size_t ws_size,
                              hipStream_t stream) {
  Params p;
  p.x = (const u16*)d_in[0];
  p.A_re = (const u16*)d_in[1]; p.A_im = (const u16*)d_in[2];
  p.B_re = (const u16*)d_in[3]; p.B_im = (const u16*)d_in[4];
  p.C_re = (const u16*)d_in[5]; p.C_im = (const u16*)d_in[6];
  p.log_dt = (const u16*)d_in[7]; p.Dskip = (const u16*)d_in[8];
  p.w1 = (const u16*)d_in[10]; p.b1 = (const u16*)d_in[11];
  p.w2 = (const u16*)d_in[12]; p.b2 = (const u16*)d_in[13];
  p.out = (u16*)d_out;
  char* ws = (char*)d_ws;
  const size_t LD = (size_t)L_ * D_ * 2;
  p.y2 = (u16*)ws; ws += LD;
  p.xnT = (u16*)ws; ws += LD;
  p.Khi = (u16*)ws; ws += LD;
  p.Klo = (u16*)ws; ws += LD;
  p.yAT = (u16*)ws; ws += LD;
  p.w1T = (u16*)ws; ws += (size_t)F_ * D_ * 2;
  p.w2T = (u16*)ws; ws += (size_t)F_ * D_ * 2;
  p.h = (u16*)ws; ws += (size_t)L_ * F_ * 2;
  kernelA<<<KA_LN_BLOCKS + KA_K_BLOCKS + KA_T1_BLOCKS + KA_T2_BLOCKS, 256, 0, stream>>>(p);
  kernelB<<<KB_G1_BLOCKS + KB_CONV_BLOCKS, 256, 0, stream>>>(p);
  kernelC<<<256, 256, 0, stream>>>(p);
}
```

```cpp
#include <hip/hip_runtime.h>
#include <hip/hip_bf16.h>
#include <stdint.h>

typedef __attribute__((ext_vector_type(8))) short bf16x8;
typedef __attribute__((ext_vector_type(4))) float f32x4;
typedef __attribute__((ext_vector_type(16))) float f32x16;
typedef unsigned short u16;

static constexpr int L_ = 2048, D_ = 512, NS_ = 128, F_ = 1792;

__device__ __forceinline__ float bf2f(u16 v) { return __uint_as_float(((unsigned)v) << 16); }
__device__ __forceinline__ u16 f2bf(float f) {
  unsigned u = __float_as_uint(f);
  u += 0x7fffu + ((u >> 16) & 1u);
  return (u16)(u >> 16);
}
__device__ __forceinline__ float rbf(float f) { return bf2f(f2bf(f)); }

__device__ __forceinline__ float wave_sum(float v) {
#pragma unroll
  for (int o = 32; o > 0; o >>= 1) v += __shfl_xor(v, o, 64);
  return v;
}

struct Params {
  const u16* x;
  const u16* A_re; const u16* A_im; const u16* B_re; const u16* B_im; const u16* C_re; const u16* C_im;
  const u16* log_dt; const u16* Dskip;
  const u16* w1; const u16* b1; const u16* w2; const u16* b2;
  u16* out;
  u16* y2;
  u16* xnT;
  u16* Khi;
  u16* Klo;
  u16* w1T;
  u16* w2T;
  u16* h;
  u16* yAT;
};

static constexpr int KA_LN_BLOCKS = 256;
static constexpr int KA_K_BLOCKS = 512;
static constexpr int KA_T1_BLOCKS = 8 * 28;
static constexpr int KA_T2_BLOCKS = 28 * 8;

__device__ void ln_role(const Params& p, int blk, char* smem) {
  u16 (*tile)[514] = (u16 (*)[514])smem;
  const int tid = threadIdx.x, lane = tid & 63, wave = tid >> 6;
  const int t0 = blk * 8;
#pragma unroll
  for (int rr = 0; rr < 2; ++rr) {
    const int r = wave * 2 + rr;
    const int t = t0 + r;
    uint4 raw = *(const uint4*)(p.x + (size_t)t * D_ + lane * 8);
    float v[8];
    const unsigned w[4] = {raw.x, raw.y, raw.z, raw.w};
#pragma unroll
    for (int i = 0; i < 4; ++i) { v[2*i] = bf2f((u16)(w[i] & 0xffff)); v[2*i+1] = bf2f((u16)(w[i] >> 16)); }
    float s = 0.f;
#pragma unroll
    for (int i = 0; i < 8; ++i) s += v[i];
    s = wave_sum(s);
    float mean = s * (1.0f / 512.0f);
    float q = 0.f;
#pragma unroll
    for (int i = 0; i < 8; ++i) { float dlt = v[i] - mean; q += dlt * dlt; }
    q = wave_sum(q);
    float m_b = rbf(mean);
    float var_b = rbf(q * (1.0f / 512.0f));
    float den = rbf(sqrtf(rbf(var_b + 1e-5f)));
    float xn[8];
#pragma unroll
    for (int i = 0; i < 8; ++i) xn[i] = rbf(rbf(v[i] - m_b) / den);
    s = 0.f;
#pragma unroll
    for (int i = 0; i < 8; ++i) s += xn[i];
    s = wave_sum(s);
    mean = s * (1.0f / 512.0f);
    q = 0.f;
#pragma unroll
    for (int i = 0; i < 8; ++i) { float dlt = xn[i] - mean; q += dlt * dlt; }
    q = wave_sum(q);
    m_b = rbf(mean);
    var_b = rbf(q * (1.0f / 512.0f));
    den = rbf(sqrtf(rbf(var_b + 1e-5f)));
    unsigned o[4];
#pragma unroll
    for (int i = 0; i < 4; ++i) {
      u16 a = f2bf(rbf(xn[2*i] - m_b) / den);
      u16 b = f2bf(rbf(xn[2*i+1] - m_b) / den);
      o[i] = (unsigned)a | ((unsigned)b << 16);
    }
    *(uint4*)(p.y2 + (size_t)t * D_ + lane * 8) = make_uint4(o[0], o[1], o[2], o[3]);
#pragma unroll
    for (int i = 0; i < 8; ++i) tile[r][lane * 8 + i] = f2bf(xn[i]);
  }
  __syncthreads();
  for (int d = tid; d < D_; d += 256) {
    unsigned o[4];
#pragma unroll
    for (int i = 0; i < 4; ++i) o[i] = (unsigned)tile[2*i][d] | ((unsigned)tile[2*i+1][d] << 16);
    uint4* dst = (uint4*)(p.xnT + (size_t)d * L_ + t0);
    dst[0] = make_uint4(o[0], o[1], o[2], o[3]);
  }
}

__device__ __forceinline__ float hi_l(int hidx) {
  if (hidx < 16) return (float)(16 * hidx);
  if (hidx < 24) return (float)(256 + 32 * (hidx - 16));
  if (hidx < 32) return (float)(512 + 64 * (hidx - 24));
  if (hidx < 40) return (float)(1024 + 128 * (hidx - 32));
  return 2048.f;
}

__device__ __forceinline__ void cexpf_fast(float zr, float zi, float l, float& er, float& ei) {
  float a = zr * l;
  float th = zi * l;
  float m = __expf(a);
  float s, c;
  __sincosf(th, &s, &c);
  er = m * c; ei = m * s;
}

__device__ void kgen_role(const Params& p, int d, char* smem) {
  float* Hs = (float*)smem;
  float2* zs = (float2*)(smem + 41 * 260 * 4);
  float2* ws = zs + 128;
  float* Kd = (float*)(ws + 128);
  const int tid = threadIdx.x, lane = tid & 63, wave = tid >> 6;
  const float dt = rbf(expf(bf2f(p.log_dt[d])));
  if (tid < 128) {
    const int n = tid;
    const size_t o = (size_t)d * NS_ + n;
    float ar = bf2f(p.A_re[o]), ai = bf2f(p.A_im[o]);
    float br = bf2f(p.B_re[o]), bi = bf2f(p.B_im[o]);
    float cr = bf2f(p.C_re[o]), ci = bf2f(p.C_im[o]);
    float zr = dt * ar, zi = dt * ai;
    float dbr = dt * br, dbi = dt * bi;
    zs[n] = make_float2(zr, zi);
    ws[n] = make_float2(cr * dbr - ci * dbi, cr * dbi + ci * dbr);
  }
  __syncthreads();
  for (int e = tid; e < 128 * 41; e += 256) {
    int n = e & 127, hidx = e >> 7;
    float2 z = zs[n], w = ws[n];
    float er, ei;
    cexpf_fast(z.x, z.y, hi_l(hidx), er, ei);
    *(float2*)(Hs + hidx * 260 + 2 * n) = make_float2(w.x * er - w.y * ei, -(w.x * ei + w.y * er));
  }
  __syncthreads();
  {
    const int seg = wave, b = lane & 15, q = lane >> 4;
    const float lo_l = (float)((1 << seg) * b);
    const int hbase = (seg == 0) ? 0 : (8 + 8 * seg);
    const int arow = (seg == 0) ? b : (b & 7);
    const float* Ap = Hs + (hbase + arow) * 260 + q;
    f32x4 acc = {0.f, 0.f, 0.f, 0.f};
#pragma unroll 4
    for (int kk = 0; kk < 64; ++kk) {
      const int n = 2 * kk + (q >> 1);
      float2 z = zs[n];
      float gr, gi;
      cexpf_fast(z.x, z.y, lo_l, gr, gi);
      float bv = (q & 1) ? gi : gr;
      float av = Ap[4 * kk];
      acc = __builtin_amdgcn_mfma_f32_16x16x4f32(av, bv, acc, 0, 0, 0);
    }
#pragma unroll
    for (int r = 0; r < 4; ++r) Kd[seg * 256 + (q * 4 + r) * 16 + b] = acc[r];
    if (wave == 0) {
      float v = Hs[40 * 260 + 2 * lane] + Hs[40 * 260 + 128 + 2 * lane];
      v = wave_sum(v);
      if (lane == 0) Kd[1024] = v;
    }
  }
  __syncthreads();
  {
    const int l0 = tid * 8;
    unsigned oh[4], ol[4];
#pragma unroll
    for (int i = 0; i < 8; ++i) {
      const int l = l0 + i;
      const int lb = (int)rbf((float)l);
      int idx;
      if (lb < 256) idx = lb;
      else if (lb < 512) idx = 256 + ((lb - 256) >> 1);
      else if (lb < 1024) idx = 512 + ((lb - 512) >> 2);
      else if (lb < 2048) idx = 768 + ((lb - 1024) >> 3);
      else idx = -1;
      float kv;
      if (idx < 0) kv = Kd[1024];
      else {
        int sg = idx >> 8, q = idx & 255;
        kv = Kd[sg * 256 + q];
      }
      u16 hi = f2bf(kv);
      u16 lo = f2bf(kv - bf2f(hi));
      if (i & 1) { oh[i >> 1] |= (unsigned)hi << 16; ol[i >> 1] |= (unsigned)lo << 16; }
      else { oh[i >> 1] = hi; ol[i >> 1] = lo; }
    }
    *(uint4*)(p.Khi + (size_t)d * L_ + l0) = make_uint4(oh[0], oh[1], oh[2], oh[3]);
    *(uint4*)(p.Klo + (size_t)d * L_ + l0) = make_uint4(ol[0], ol[1], ol[2], ol[3]);
  }
}

__device__ void transpose_role(const u16* src, u16* dst, int R, int C, int r0, int c0, char* smem) {
  u16 (*tile)[66] = (u16 (*)[66])smem;
  const int tid = threadIdx.x;
  for (int i = tid; i < 64 * 8; i += 256) {
    int r = i >> 3, c8 = (i & 7) * 8;
    uint4 v = *(const uint4*)(src + (size_t)(r0 + r) * C + c0 + c8);
    const unsigned w[4] = {v.x, v.y, v.z, v.w};
#pragma unroll
    for (int k = 0; k < 4; ++k) { tile[r][c8 + 2*k] = (u16)(w[k] & 0xffff); tile[r][c8 + 2*k + 1] = (u16)(w[k] >> 16); }
  }
  __syncthreads();
  for (int i = tid; i < 64 * 8; i += 256) {
    int c = i >> 3, r8 = (i & 7) * 8;
    unsigned o[4];
#pragma unroll
    for (int k = 0; k < 4; ++k) o[k] = (unsigned)tile[r8 + 2*k][c] | ((unsigned)tile[r8 + 2*k + 1][c] << 16);
    *(uint4*)(dst + (size_t)(c0 + c) * R + r0 + r8) = make_uint4(o[0], o[1], o[2], o[3]);
  }
}

__global__ void __launch_bounds__(256) kernelA(Params p) {
  __shared__ __attribute__((aligned(16))) char smem[49152];
  int blk = blockIdx.x;
  if (blk < KA_LN_BLOCKS) { ln_role(p, blk, smem); return; }
  blk -= KA_LN_BLOCKS;
  if (blk < KA_K_BLOCKS) { kgen_role(p, blk, smem); return; }
  blk -= KA_K_BLOCKS;
  if (blk < KA_T1_BLOCKS) { transpose_role(p.w1, p.w1T, D_, F_, (blk / 28) * 64, (blk % 28) * 64, smem); return; }
  blk -= KA_T1_BLOCKS;
  transpose_role(p.w2, p.w2T, F_, D_, (blk / 8) * 64, (blk % 8) * 64, smem);
}

static constexpr int KS_ = 2192;
static constexpr int CONV_K_BYTES = 8 * KS_ * 2;
static constexpr int CONV_U_OFF = 2 * CONV_K_BYTES;
static constexpr int CONV_LDS = CONV_U_OFF + 4096 * 2;

__device__ void conv_role(const Params& p, int d, char* smem) {
  const int tid = threadIdx.x, lane = tid & 63, wave = tid >> 6;
  u16* U = (u16*)(smem + CONV_U_OFF);
  for (int i = tid; i < 512; i += 256) {
    uint4 v = (i < 256) ? make_uint4(0, 0, 0, 0) : *(const uint4*)(p.xnT + (size_t)d * L_ + (i - 256) * 8);
    *(uint4*)(U + i * 8) = v;
  }
  for (int it = tid; it < 2 * 262; it += 256) {
    const int which = it >= 262;
    const int q = which ? it - 262 : it;
    const u16* Ksrc = (which ? p.Klo : p.Khi) + (size_t)d * L_;
    unsigned w[8];
    {
      int ch = 255 - q;
      uint4 v = (ch >= 0) ? *(const uint4*)(Ksrc + ch * 8) : make_uint4(0, 0, 0, 0);
      w[0] = __builtin_amdgcn_alignbit(v.w, v.w, 16);
      w[1] = __builtin_amdgcn_alignbit(v.z, v.z, 16);
      w[2] = __builtin_amdgcn_alignbit(v.y, v.y, 16);
      w[3] = __builtin_amdgcn_alignbit(v.x, v.x, 16);
      ch = 254 - q;
      v = (ch >= 0) ? *(const uint4*)(Ksrc + ch * 8) : make_uint4(0, 0, 0, 0);
      w[4] = __builtin_amdgcn_alignbit(v.w, v.w, 16);
      w[5] = __builtin_amdgcn_alignbit(v.z, v.z, 16);
      w[6] = __builtin_amdgcn_alignbit(v.y, v.y, 16);
      w[7] = __builtin_amdgcn_alignbit(v.x, v.x, 16);
    }
    char* base = smem + which * CONV_K_BYTES + q * 16;
#pragma unroll
    for (int r = 0; r < 8; ++r) {
      uint4 o;
      const int k = r >> 1;
      if ((r & 1) == 0) o = make_uint4(w[k], w[k + 1], w[k + 2], w[k + 3]);
      else o = make_uint4(__builtin_amdgcn_alignbit(w[k + 1], w[k], 16), __builtin_amdgcn_alignbit(w[k + 2], w[k + 1], 16),
                          __builtin_amdgcn_alignbit(w[k + 3], w[k + 2], 16), __builtin_amdgcn_alignbit(w[k + 4], w[k + 3], 16));
      *(uint4*)(base + r * (KS_ * 2)) = o;
    }
  }
  __syncthreads();
  const int b = lane & 31, hh = lane >> 5;
  const int r = (7 - b) & 7;
  f32x16 acc0, acc1;
#pragma unroll
  for (int i = 0; i < 16; ++i) { acc0[i] = 0.f; acc1[i] = 0.f; }
  const char* abase = smem + r * (KS_ * 2) + (2047 - b - r + 8 * hh) * 2;
  const u16* ub0 = U + 2048 + 32 * b + 8 * hh;
  const u16* ub1 = U + 2048 + 32 * (32 + b) + 8 * hh;
  for (int j = wave; j < 64; j += 4) {
#pragma unroll
    for (int half = 0; half < 2; ++half) {
      bf16x8 ahi = *(const bf16x8*)(abase - 64 * j + 32 * half);
      bf16x8 alo = *(const bf16x8*)(abase - 64 * j + 32 * half + CONV_K_BYTES);
      bf16x8 b1v = *(const bf16x8*)(ub1 - 32 * j + 16 * half);
      acc1 = __builtin_amdgcn_mfma_f32_32x32x16_bf16(ahi, b1v, acc1, 0, 0, 0);
      acc1 = __builtin_amdgcn_mfma_f32_32x32x16_bf16(alo, b1v, acc1, 0, 0, 0);
      if (j < 32) {
        bf16x8 b0v = *(const bf16x8*)(ub0 - 32 * j + 16 * half);
        acc0 = __builtin_amdgcn_mfma_f32_32x32x16_bf16(ahi, b0v, acc0, 0, 0, 0);
        acc0 = __builtin_amdgcn_mfma_f32_32x32x16_bf16(alo, b0v, acc0, 0, 0, 0);
      }
    }
  }
  __syncthreads();
  float* red = (float*)smem;
#pragma unroll
  for (int reg = 0; reg < 16; ++reg) {
    int row = (reg & 3) + 8 * (reg >> 2) + 4 * hh;
    red[(wave * 64 + b) * 33 + row] = acc0[reg];
    red[(wave * 64 + 32 + b) * 33 + row] = acc1[reg];
  }
  __syncthreads();
  const float dsk = bf2f(p.Dskip[d]);
  {
    const int t0 = tid * 8;
    unsigned o[4];
#pragma unroll
    for (int i = 0; i < 8; ++i) {
      const int t = t0 + i, a = t >> 5, bb = t & 31;
      float s = red[a * 33 + bb] + red[(64 + a) * 33 + bb] + red[(128 + a) * 33 + bb] + red[(192 + a) * 33 + bb];
      float u = bf2f(U[2048 + t]);
      u16 y = f2bf(rbf(s) + rbf(u * dsk));
      if (i & 1) o[i >> 1] |= (unsigned)y << 16; else o[i >> 1] = y;
    }
    *(uint4*)(p.yAT + (size_t)d * L_ + t0) = make_uint4(o[0], o[1], o[2], o[3]);
  }
}

__device__ __forceinline__ float gelu_tanh(float x) {
  const float k0 = 0.7978845608028654f, k1 = 0.044715f;
  float inner = k0 * (x + k1 * x * x * x);
  float e = __expf(2.0f * inner);
  float th = 1.0f - __fdividef(2.0f, e + 1.0f);
  return 0.5f * x * (1.0f + th);
}

static constexpr int G1_LDR = 72;
__device__ void gemm1_role(const Params& p, int blk, char* smem) {
  const int tid = threadIdx.x, lane = tid & 63, wave = tid >> 6;
  const int m0 = (blk / 14) * 128, n0 = (blk % 14) * 128;
  u16* sA = (u16*)smem;
  u16* sB = sA + 2 * 128 * G1_LDR;
  const int wm = (wave >> 1) * 64, wn = (wave & 1) * 64;
  const int r = lane & 31, hh = lane >> 5;
  f32x16 acc[2][2];
#pragma unroll
  for (int i = 0; i < 2; ++i)
#pragma unroll
    for (int j = 0; j < 2; ++j)
#pragma unroll
      for (int k = 0; k < 16; ++k) acc[i][j][k] = 0.f;
  const u16* Ag = p.y2 + (size_t)m0 * D_;
  const u16* Bg = p.w1T + (size_t)n0 * D_;
  const int lrow = tid >> 3, lcol = (tid & 7) * 8;
  uint4 a0_0, a0_1, a0_2, a0_3, b0_0, b0_1, b0_2, b0_3, a1_0, a1_1, a1_2, a1_3, b1_0, b1_1, b1_2, b1_3;
#define G1_LOAD(S, KT) do { int kk_ = (KT) < 8 ? (KT) : 7; \
    const u16* ap_ = Ag + (size_t)lrow * D_ + kk_ * 64 + lcol; const u16* bp_ = Bg + (size_t)lrow * D_ + kk_ * 64 + lcol; \
    a##S##_0 = *(const uint4*)(ap_); a##S##_1 = *(const uint4*)(ap_ + 32 * D_); a##S##_2 = *(const uint4*)(ap_ + 64 * D_); a##S##_3 = *(const uint4*)(ap_ + 96 * D_); \
    b##S##_0 = *(const uint4*)(bp_); b##S##_1 = *(const uint4*)(bp_ + 32 * D_); b##S##_2 = *(const uint4*)(bp_ + 64 * D_); b##S##_3 = *(const uint4*)(bp_ + 96 * D_); } while (0)
#define G1_STORE(S, BUF) do { u16* da_ = sA + (BUF) * 128 * G1_LDR + lrow * G1_LDR + lcol; u16* db_ = sB + (BUF) * 128 * G1_LDR + lrow * G1_LDR + lcol; \
    *(uint4*)(da_) = a##S##_0; *(uint4*)(da_ + 32 * G1_LDR) = a##S##_1; *(uint4*)(da_ + 64 * G1_LDR) = a##S##_2; *(uint4*)(da_ + 96 * G1_LDR) = a##S##_3; \
    *(uint4*)(db_) = b##S##_0; *(uint4*)(db_ + 32 * G1_LDR) = b##S##_1; *(uint4*)(db_ + 64 * G1_LDR) = b##S##_2; *(uint4*)(db_ + 96 * G1_LDR) = b##S##_3; } while (0)
#define G1_COMPUTE(BUF) do { const u16* cA = sA + (BUF) * 128 * G1_LDR; const u16* cB = sB + (BUF) * 128 * G1_LDR; \
    _Pragma("unroll") for (int ks = 0; ks < 4; ++ks) { \
      bf16x8 fa0 = *(const bf16x8*)(cA + (wm + r) * G1_LDR + ks * 16 + hh * 8); \
      bf16x8 fa1 = *(const bf16x8*)(cA + (wm + 32 + r) * G1_LDR + ks * 16 + hh * 8); \
      bf16x8 fb0 = *(const bf16x8*)(cB + (wn + r) * G1_LDR + ks * 16 + hh * 8); \
      bf16x8 fb1 = *(const bf16x8*)(cB + (wn + 32 + r) * G1_LDR + ks * 16 + hh * 8); \
      acc[0][0] = __builtin_amdgcn_mfma_f32_32x32x16_bf16(fa0, fb0, acc[0][0], 0, 0, 0); \
      acc[0][1] = __builtin_amdgcn_mfma_f32_32x32x16_bf16(fa0, fb1, acc[0][1], 0, 0, 0); \
      acc[1][0] = __builtin_amdgcn_mfma_f32_32x32x16_bf16(fa1, fb0, acc[1][0], 0, 0, 0); \
      acc[1][1] = __builtin_amdgcn_mfma_f32_32x32x16_bf16(fa1, fb1, acc[1][1], 0, 0, 0); } } while (0)
  G1_LOAD(0, 0); G1_LOAD(1, 1);
  G1_STORE(0, 0);
  __syncthreads();
  for (int kt = 0; kt < 8; kt += 2) {
    G1_LOAD(0, kt + 2);
    G1_COMPUTE(0); G1_STORE(1, 1); __syncthreads();
    G1_LOAD(1, kt + 3);
    G1_COMPUTE(1); G1_STORE(0, 0); __syncthreads();
  }
  u16* sC = (u16*)smem;
  const int LDC = 136;
#pragma unroll
  for (int j = 0; j < 2; ++j) {
    const int col = wn + j * 32 + r;
    const float bias = bf2f(p.b1[n0 + col]);
#pragma unroll
    for (int i = 0; i < 2; ++i)
#pragma unroll
      for (int reg = 0; reg < 16; ++reg) {
        int row = wm + i * 32 + (reg & 3) + 8 * (reg >> 2) + 4 * hh;
        float v = rbf(rbf(acc[i][j][reg]) + bias);
        sC[row * LDC + col] = f2bf(gelu_tanh(v));
      }
  }
  __syncthreads();
  for (int c = tid; c < 128 * 16; c += 256) {
    int row = c >> 4, col = (c & 15) * 8;
    *(uint4*)(p.h + (size_t)(m0 + row) * F_ + n0 + col) = *(const uint4*)(sC + row * LDC + col);
  }
}

static constexpr int KB_CONV_BLOCKS = 512;
static constexpr int KB_G1_BLOCKS = 16 * 14;
__global__ void __launch_bounds__(256) kernelB(Params p) {
  __shared__ __attribute__((aligned(16))) char smem[CONV_LDS];
  int blk = blockIdx.x;
  if (blk < KB_G1_BLOCKS) { gemm1_role(p, blk, smem); return; }
  blk -= KB_G1_BLOCKS;
  conv_role(p, blk, smem);
}

__global__ void __launch_bounds__(256) kernelC(Params p) {
  __shared__ __attribute__((aligned(16))) u16 smem[2 * 2 * 64 * G1_LDR];
  const int tid = threadIdx.x, lane = tid & 63, wave = tid >> 6;
  const int m0 = (blockIdx.x >> 3) * 64, n0 = (blockIdx.x & 7) * 64;
  u16* sA = smem;
  u16* sB = smem + 2 * 64 * G1_LDR;
  const int wm = (wave >> 1) * 32, wn = (wave & 1) * 32;
  const int r = lane & 31, hh = lane >> 5;
  f32x16 acc;
#pragma unroll
  for (int k = 0; k < 16; ++k) acc[k] = 0.f;
  uint4 ra0_0, ra0_1, rb0_0, rb0_1, ra1_0, ra1_1, rb1_0, rb1_1, ra2_0, ra2_1, rb2_0, rb2_1;
  const int lrow = tid >> 3, lcol = (tid & 7) * 8;
  const u16* Ag = p.h + (size_t)m0 * F_;
  const u16* Bg = p.w2T + (size_t)n0 * F_;
  const int NK = F_ / 64;
#define G2_LOAD(S, KT) do { int kk_ = (KT) < NK ? (KT) : NK - 1; \
      ra##S##_0 = *(const uint4*)(Ag + (size_t)lrow * F_ + kk_ * 64 + lcol); \
      ra##S##_1 = *(const uint4*)(Ag + (size_t)(lrow + 32) * F_ + kk_ * 64 + lcol); \
      rb##S##_0 = *(const uint4*)(Bg + (size_t)lrow * F_ + kk_ * 64 + lcol); \
      rb##S##_1 = *(const uint4*)(Bg + (size_t)(lrow + 32) * F_ + kk_ * 64 + lcol); } while (0)
#define G2_STORE(S, BUF) do { \
      *(uint4*)(sA + (BUF) * 64 * G1_LDR + lrow * G1_LDR + lcol) = ra##S##_0; \
      *(uint4*)(sA + (BUF) * 64 * G1_LDR + (lrow + 32) * G1_LDR + lcol) = ra##S##_1; \
      *(uint4*)(sB + (BUF) * 64 * G1_LDR + lrow * G1_LDR + lcol) = rb##S##_0; \
      *(uint4*)(sB + (BUF) * 64 * G1_LDR + (lrow + 32) * G1_LDR + lcol) = rb##S##_1; } while (0)
#define G2_COMPUTE(BUF) do { const u16* cA = sA + (BUF) * 64 * G1_LDR; const u16* cB = sB + (BUF) * 64 * G1_LDR; \
    _Pragma("unroll") for (int ks = 0; ks < 4; ++ks) { \
      bf16x8 a0 = *(const bf16x8*)(cA + (wm + r) * G1_LDR + ks * 16 + hh * 8); \
      bf16x8 b0 = *(const bf16x8*)(cB + (wn + r) * G1_LDR + ks * 16 + hh * 8); \
      acc = __builtin_amdgcn_mfma_f32_32x32x16_bf16(a0, b0, acc, 0, 0, 0); } } while (0)
  G2_LOAD(0, 0); G2_LOAD(1, 1); G2_LOAD(2, 2);
  G2_STORE(0, 0);
  __syncthreads();
  for (int kt = 0; kt < 27; kt += 3) {
    G2_LOAD(0, kt + 3);
    G2_COMPUTE(kt & 1); G2_STORE(1, (kt + 1) & 1); __syncthreads();
    G2_LOAD(1, kt + 4);
    G2_COMPUTE((kt + 1) & 1); G2_STORE(2, kt & 1); __syncthreads();
    G2_LOAD(2, kt + 5);
    G2_COMPUTE(kt & 1); G2_STORE(0, (kt + 1) & 1); __syncthreads();
  }
  G2_COMPUTE(1);
  __syncthreads();
  float* sC = (float*)smem;
  {
    const int dd = n0 + wn + r;
    const float bias = bf2f(p.b2[dd]);
#pragma unroll
    for (int reg = 0; reg < 16; ++reg) {
      int row = wm + (reg & 3) + 8 * (reg >> 2) + 4 * hh;
      sC[row * 68 + wn + r] = rbf(rbf(acc[reg]) + bias);
    }
  }
  u16* sY = (u16*)(sC + 64 * 68);
  __syncthreads();
  for (int c = tid; c < 64 * 8; c += 256) {
    int dd = c >> 3, t8 = (c & 7) * 8;
    *(uint4*)(sY + dd * 72 + t8) = *(const uint4*)(p.yAT + (size_t)(n0 + dd) * L_ + m0 + t8);
  }
  __syncthreads();
  for (int c = tid; c < 64 * 8; c += 256) {
    int t = c >> 3, d8 = (c & 7) * 8;
    uint4 xv = *(const uint4*)(p.x + (size_t)(m0 + t) * D_ + n0 + d8);
    const unsigned xw[4] = {xv.x, xv.y, xv.z, xv.w};
    unsigned o[4];
#pragma unroll
    for (int i = 0; i < 8; ++i) {
      float xf = bf2f((u16)((xw[i >> 1] >> (16 * (i & 1))) & 0xffff));
      float yA = bf2f(sY[(d8 + i) * 72 + t]);
      u16 ov = f2bf(rbf(xf + yA) + sC[t * 68 + d8 + i]);
      if (i & 1) o[i >> 1] |= (unsigned)ov << 16; else o[i >> 1] = ov;
    }
    *(uint4*)(p.out + (size_t)(m0 + t) * D_ + n0 + d8) = make_uint4(o[0], o[1], o[2], o[3]);
  }
}

extern "C" void kernel_launch(void* const* d_in, const int* in_sizes, int n_in,
                              void* d_out, int out_size, void* d_ws, size_t ws_size,
                              hipStream_t stream) {
  Params p;
  p.x = (const u16*)d_in[0];
  p.A_re = (const u16*)d_in[1]; p.A_im = (const u16*)d_in[2];
  p.B_re = (const u16*)d_in[3]; p.B_im = (const u16*)d_in[4];
  p.C_re = (const u16*)d_in[5]; p.C_im = (const u16*)d_in[6];
  p.log_dt = (const u16*)d_in[7]; p.Dskip = (const u16*)d_in[8];
  p.w1 = (const u16*)d_in[10]; p.b1 = (const u16*)d_in[11];
  p.w2 = (const u16*)d_in[12]; p.b2 = (const u16*)d_in[13];
  p.out = (u16*)d_out;
  char* ws = (char*)d_ws;
  const size_t LD = (size_t)L_ * D_ * 2;
  p.y2 = (u16*)ws; ws += LD;
  p.xnT = (u16*)ws; ws += LD;
  p.Khi = (u16*)ws; ws += LD;
  p.Klo = (u16*)ws; ws += LD;
  p.yAT = (u16*)ws; ws += LD;
  p.w1T = (u16*)ws; ws += (size_t)F_ * D_ * 2;
  p.w2T = (u16*)ws; ws += (size_t)F_ * D_ * 2;
  p.h = (u16*)ws; ws += (size_t)L_ * F_ * 2;
  kernelA<<<KA_LN_BLOCKS + KA_K_BLOCKS + KA_T1_BLOCKS + KA_T2_BLOCKS, 256, 0, stream>>>(p);
  kernelB<<<KB_G1_BLOCKS + KB_CONV_BLOCKS, 256, 0, stream>>>(p);
  kernelC<<<256, 256, 0, stream>>>(p);
}
```
